```python
import torch

def _softmax(x, axis=-1):
    return torch.softmax(x, dim=axis)
def _gelu(x):
    return torch.nn.functional.gelu(x, approximate="tanh")
def _silu(x):
    return torch.nn.functional.silu(x)
def _sigmoid(x):
    return torch.sigmoid(x)
def _layernorm(x, axis=-1, eps=1e-5):
    m = x.mean(dim=axis, keepdim=True)
    v = x.var(dim=axis, keepdim=True, unbiased=False)
    return (x - m) / torch.sqrt(v + eps)
def _scatter_add(tgt, idx, src, axis=0):
    return tgt.clone().index_add_(axis, idx, src)
def _segment_sum(src, seg, n):
    out = torch.zeros((n,) + src.shape[1:], dtype=src.dtype)
    return out.index_add_(0, seg, src)
def _pad(x, pw):
    flat = tuple(v for pair in reversed(pw) for v in pair)
    return torch.nn.functional.pad(x, flat)

kernel_name = 'standalone__low_rank_proj_linformer__decode'

def setup_inputs():

    _w = (lambda a: (lambda t: t.to(torch.bfloat16) if t.is_floating_point() else t)(torch.as_tensor(a)))
    B, H, Sq, Skv, Dh, R = 2, 32, 1, 8192, 128, 64
    return {
        "q": _w((lambda *s: torch.randn(*s, dtype=torch.bfloat16) * 0.8)(B, H, Sq, Dh)),
        "k": _w((lambda *s: torch.randn(*s, dtype=torch.bfloat16) * 0.8)(B, H, Skv, Dh)),
        "v": _w((lambda *s: torch.randn(*s, dtype=torch.bfloat16) * 0.05)(B, H, Skv, Dh)),
        "e_proj": _w((lambda *s: torch.randn(*s, dtype=torch.bfloat16) * 0.8)(R, Skv) * (Skv ** -0.5)),
        "f_proj": _w((lambda *s: torch.randn(*s, dtype=torch.bfloat16) * 0.05)(R, Skv) * (Skv ** -0.5)),
    }

def reference(q, k, v, e_proj, f_proj):
    B, H, Sq, Dh = q.shape
    kp = torch.einsum("rn,bhnd->bhrd", e_proj, k)
    vp = torch.einsum("rn,bhnd->bhrd", f_proj, v)
    scores = torch.einsum("bhqd,bhrd->bhqr", q, kp) * (Dh ** -0.5)
    p = _softmax(scores, axis=-1)
    o = torch.einsum("bhqr,bhrd->bhqd", p, vp)
    return o
```

```cpp
#include <hip/hip_runtime.h>
#include <hip/hip_bf16.h>
#include <stdint.h>

namespace {

constexpr int BH = 64;
constexpr int SKV = 8192;
constexpr int DH = 128;
constexpr int RR = 64;
constexpr float SCALE = 0.08838834764831845f;

constexpr int S1 = 16;
constexpr int S2 = 16;
constexpr int NWAVES = 4;

constexpr size_t WS_PS = 0;
constexpr size_t WS_PO = (size_t)1 << 20;
constexpr size_t WS_END = WS_PO + (size_t)BH * S2 * DH * 4;

typedef __attribute__((ext_vector_type(4))) unsigned u32x4;
typedef __attribute__((ext_vector_type(4))) float f32x4;
typedef __attribute__((ext_vector_type(2))) __bf16 bf16x2;

__device__ __forceinline__ float dot2(unsigned a, unsigned b, float acc) {
  return __builtin_amdgcn_fdot2_f32_bf16(__builtin_bit_cast(bf16x2, a), __builtin_bit_cast(bf16x2, b), acc, false);
}
template <int CTRL> __device__ __forceinline__ float dpp(float x) {
  return __builtin_bit_cast(float, __builtin_amdgcn_mov_dpp(__builtin_bit_cast(int, x), CTRL, 0xf, 0xf, true));
}
constexpr int DPP_XOR1 = 0xB1, DPP_XOR2 = 0x4E, DPP_XOR7 = 0x141, DPP_ROR8 = 0x128;

__device__ __forceinline__ float bf_lo(unsigned u) { return __builtin_bit_cast(float, u << 16); }
__device__ __forceinline__ float bf_hi(unsigned u) { return __builtin_bit_cast(float, u & 0xffff0000u); }

__device__ __forceinline__ void wave_lds_sync() {
  __builtin_amdgcn_fence(__ATOMIC_RELEASE, "wavefront");
  __builtin_amdgcn_wave_barrier();
  __builtin_amdgcn_fence(__ATOMIC_ACQUIRE, "wavefront");
}

__device__ __forceinline__ float wave_max(float x) {
#pragma unroll
  for (int o = 32; o > 0; o >>= 1) x = fmaxf(x, __shfl_xor(x, o, 64));
  return x;
}
__device__ __forceinline__ float wave_sum(float x) {
#pragma unroll
  for (int o = 32; o > 0; o >>= 1) x += __shfl_xor(x, o, 64);
  return x;
}

__global__ void __launch_bounds__(256) kphase_kernel(const uint16_t* __restrict__ q, const uint16_t* __restrict__ k,
                                                     const uint16_t* __restrict__ e, float* __restrict__ ps) {
  constexpr int ROWS_WG = SKV / S1, ROWS_WAVE = ROWS_WG / NWAVES, NBLK = ROWS_WAVE / 64;
  static_assert(ROWS_WAVE % 64 == 0, "whole 64-row blocks per wave");
  __shared__ __attribute__((aligned(16))) float s_lds[NWAVES][64];
  __shared__ __attribute__((aligned(16))) float red[NWAVES][64][8];
  const int bh = blockIdx.x / S1, split = blockIdx.x % S1;
  const int tid = threadIdx.x, lane = tid & 63, wave = tid >> 6;
  const int g = lane >> 4, c = lane & 15, rs = lane >> 3, no = lane & 7;
  const u32x4 qv = *reinterpret_cast<const u32x4*>(q + bh * DH + c * 8);
  const int n0 = split * ROWS_WG + wave * ROWS_WAVE;
  const uint16_t* kb = k + ((size_t)bh * SKV + n0 + g) * DH + c * 8;
  const uint16_t* eb = e + (size_t)rs * SKV + n0 + no * 8;
  float acc[8];
#pragma unroll
  for (int j = 0; j < 8; ++j) acc[j] = 0.f;
#pragma unroll 1
  for (int blk = 0; blk < NBLK; ++blk) {
    u32x4 kv[16];
#pragma unroll
    for (int i = 0; i < 16; ++i)
      kv[i] = __builtin_nontemporal_load(reinterpret_cast<const u32x4*>(kb + (size_t)(blk * 64 + 4 * i) * DH));
    u32x4 ev[8];
#pragma unroll
    for (int j = 0; j < 8; ++j) ev[j] = *reinterpret_cast<const u32x4*>(eb + (size_t)j * 8 * SKV + blk * 64);
    float x[16];
#pragma unroll
    for (int i = 0; i < 16; ++i)
      x[i] = dot2(kv[i].w, qv.w, dot2(kv[i].z, qv.z, dot2(kv[i].y, qv.y, dot2(kv[i].x, qv.x, 0.f))));
#define TR_STEP(HALF, CTRL, BIT)                                              \
  _Pragma("unroll") for (int h = 0; h < (HALF); ++h) {                         \
    const float lo_ = x[h], hi_ = x[h + (HALF)];                               \
    const float t1_ = lo_ + dpp<CTRL>(lo_), t2_ = hi_ + dpp<CTRL>(hi_);        \
    x[h] = (c & (BIT)) ? t2_ : t1_;                                            \
  }
    TR_STEP(8, DPP_ROR8, 8) TR_STEP(4, DPP_XOR7, 4) TR_STEP(2, DPP_XOR2, 2) TR_STEP(1, DPP_XOR1, 1)
#undef TR_STEP
    s_lds[wave][4 * c + g] = x[0];
    wave_lds_sync();
    const f32x4 s0 = *reinterpret_cast<const f32x4*>(&s_lds[wave][8 * no]);
    const f32x4 s1 = *reinterpret_cast<const f32x4*>(&s_lds[wave][8 * no + 4]);
#pragma unroll
    for (int j = 0; j < 8; ++j) {
      float a = acc[j];
      a = fmaf(bf_lo(ev[j].x), s0.x, a); a = fmaf(bf_hi(ev[j].x), s0.y, a);
      a = fmaf(bf_lo(ev[j].y), s0.z, a); a = fmaf(bf_hi(ev[j].y), s0.w, a);
      a = fmaf(bf_lo(ev[j].z), s1.x, a); a = fmaf(bf_hi(ev[j].z), s1.y, a);
      a = fmaf(bf_lo(ev[j].w), s1.z, a); a = fmaf(bf_hi(ev[j].w), s1.w, a);
      acc[j] = a;
    }
    wave_lds_sync();
  }
  *reinterpret_cast<f32x4*>(&red[wave][lane][0]) = f32x4{acc[0], acc[1], acc[2], acc[3]};
  *reinterpret_cast<f32x4*>(&red[wave][lane][4]) = f32x4{acc[4], acc[5], acc[6], acc[7]};
  __syncthreads();
  if (tid < RR) {
    const int j = tid >> 3, r7 = tid & 7;
    float s = 0.f;
#pragma unroll
    for (int w = 0; w < NWAVES; ++w)
#pragma unroll
      for (int u = 0; u < 8; ++u) s += red[w][r7 * 8 + u][j];
    ps[((size_t)bh * S1 + split) * RR + tid] = s;
  }
}

__global__ void __launch_bounds__(256) vphase_kernel(const uint16_t* __restrict__ v, const uint16_t* __restrict__ f,
                                                     const float* __restrict__ ps, float* __restrict__ po) {
  constexpr int ROWS_WG = SKV / S2, ROWS_WAVE = ROWS_WG / NWAVES, NBLK = ROWS_WAVE / 64;
  static_assert(ROWS_WAVE % 64 == 0, "whole 64-row blocks per wave");
  __shared__ __attribute__((aligned(16))) float p_lds[NWAVES][64];
  __shared__ __attribute__((aligned(16))) float w_lds[NWAVES][64];
  __shared__ __attribute__((aligned(16))) float red[NWAVES][4][DH];
  const int bh = blockIdx.x / S2, split = blockIdx.x % S2;
  const int tid = threadIdx.x, lane = tid & 63, wave = tid >> 6;
  const int g = lane >> 4, c = lane & 15, rs = lane >> 3, no = lane & 7;
  const int n0 = split * ROWS_WG + wave * ROWS_WAVE;
  const uint16_t* vb = v + ((size_t)bh * SKV + n0 + g) * DH + c * 8;
  const uint16_t* fb = f + (size_t)rs * SKV + n0 + no * 8;

  u32x4 vv[16];
  u32x4 fv[8];
#pragma unroll
  for (int i = 0; i < 16; ++i) vv[i] = __builtin_nontemporal_load(reinterpret_cast<const u32x4*>(vb + (size_t)(4 * i) * DH));
#pragma unroll
  for (int j = 0; j < 8; ++j) fv[j] = *reinterpret_cast<const u32x4*>(fb + (size_t)j * 8 * SKV);

  float sc = 0.f;
#pragma unroll
  for (int s = 0; s < S1; ++s) sc += ps[((size_t)bh * S1 + s) * RR + lane];
  sc *= SCALE;
  const float m = wave_max(sc);
  const float ex = __builtin_amdgcn_exp2f((sc - m) * 1.4426950408889634f);
  const float tot = wave_sum(ex);
  p_lds[wave][lane] = ex / tot;
  wave_lds_sync();
  float preg[8];
#pragma unroll
  for (int j = 0; j < 8; ++j) preg[j] = p_lds[wave][8 * j + rs];

  const int tsel = ((lane >> 5) & 1) + 2 * ((lane >> 4) & 1) + 4 * ((lane >> 3) & 1);
  const int wpos = (tsel & 3) * 16 + 2 * no + (tsel >> 2);

  float acc[8];
#pragma unroll
  for (int t = 0; t < 8; ++t) acc[t] = 0.f;
#pragma unroll 1
  for (int blk = 0; blk < NBLK; ++blk) {
    if (blk > 0) {
#pragma unroll
      for (int i = 0; i < 16; ++i)
        vv[i] = __builtin_nontemporal_load(reinterpret_cast<const u32x4*>(vb + (size_t)(blk * 64 + 4 * i) * DH));
#pragma unroll
      for (int j = 0; j < 8; ++j) fv[j] = *reinterpret_cast<const u32x4*>(fb + (size_t)j * 8 * SKV + blk * 64);
    }
    float wp[8];
#pragma unroll
    for (int t = 0; t < 8; ++t) wp[t] = 0.f;
#pragma unroll
    for (int j = 0; j < 8; ++j) {
      wp[0] = fmaf(bf_lo(fv[j].x), preg[j], wp[0]); wp[1] = fmaf(bf_hi(fv[j].x), preg[j], wp[1]);
      wp[2] = fmaf(bf_lo(fv[j].y), preg[j], wp[2]); wp[3] = fmaf(bf_hi(fv[j].y), preg[j], wp[3]);
      wp[4] = fmaf(bf_lo(fv[j].z), preg[j], wp[4]); wp[5] = fmaf(bf_hi(fv[j].z), preg[j], wp[5]);
      wp[6] = fmaf(bf_lo(fv[j].w), preg[j], wp[6]); wp[7] = fmaf(bf_hi(fv[j].w), preg[j], wp[7]);
    }
    float y[4];
#pragma unroll
    for (int h = 0; h < 4; ++h) {
      const float lo_ = wp[h], hi_ = wp[h + 4];
      const float t1_ = lo_ + dpp<DPP_ROR8>(lo_), t2_ = hi_ + dpp<DPP_ROR8>(hi_);
      y[h] = (lane & 8) ? t2_ : t1_;
    }
    float z[2];
#pragma unroll
    for (int h = 0; h < 2; ++h) {
      auto sw = __builtin_amdgcn_permlane16_swap(__float_as_uint(y[h]), __float_as_uint(y[h + 2]), false, false);
      z[h] = __uint_as_float(sw[0]) + __uint_as_float(sw[1]);
    }
    auto sw32 = __builtin_amdgcn_permlane32_swap(__float_as_uint(z[0]), __float_as_uint(z[1]), false, false);
    w_lds[wave][wpos] = __uint_as_float(sw32[0]) + __uint_as_float(sw32[1]);
    wave_lds_sync();
    f32x4 wr[4];
#pragma unroll
    for (int u = 0; u < 4; ++u) wr[u] = *reinterpret_cast<const f32x4*>(&w_lds[wave][g * 16 + 4 * u]);
#pragma unroll
    for (int i = 0; i < 16; ++i) {
      const float wv = wr[i >> 2][i & 3];
      acc[0] = fmaf(bf_lo(vv[i].x), wv, acc[0]); acc[1] = fmaf(bf_hi(vv[i].x), wv, acc[1]);
      acc[2] = fmaf(bf_lo(vv[i].y), wv, acc[2]); acc[3] = fmaf(bf_hi(vv[i].y), wv, acc[3]);
      acc[4] = fmaf(bf_lo(vv[i].z), wv, acc[4]); acc[5] = fmaf(bf_hi(vv[i].z), wv, acc[5]);
      acc[6] = fmaf(bf_lo(vv[i].w), wv, acc[6]); acc[7] = fmaf(bf_hi(vv[i].w), wv, acc[7]);
    }
    wave_lds_sync();
  }
  *reinterpret_cast<f32x4*>(&red[wave][g][8 * c]) = f32x4{acc[0], acc[1], acc[2], acc[3]};
  *reinterpret_cast<f32x4*>(&red[wave][g][8 * c + 4]) = f32x4{acc[4], acc[5], acc[6], acc[7]};
  __syncthreads();
  if (tid < DH) {
    float s = 0.f;
#pragma unroll
    for (int w = 0; w < NWAVES; ++w)
#pragma unroll
      for (int u = 0; u < 4; ++u) s += red[w][u][tid];
    po[((size_t)bh * S2 + split) * DH + tid] = s;
  }
}

__global__ void __launch_bounds__(DH) combine_kernel(const float* __restrict__ po, __hip_bfloat16* __restrict__ out) {
  const int bh = blockIdx.x, d = threadIdx.x;
  float s = 0.f;
#pragma unroll
  for (int i = 0; i < S2; ++i) s += po[((size_t)bh * S2 + i) * DH + d];
  out[bh * DH + d] = __float2bfloat16(s);
}

}

extern "C" void kernel_launch(void* const* d_in, const int* in_sizes, int n_in,
                              void* d_out, int out_size, void* d_ws, size_t ws_size,
                              hipStream_t stream) {
  if (n_in != 5 || in_sizes[0] != BH * DH || in_sizes[1] != BH * SKV * DH || in_sizes[2] != BH * SKV * DH ||
      in_sizes[3] != RR * SKV || in_sizes[4] != RR * SKV || out_size != BH * DH || ws_size < WS_END)
    return;
  const uint16_t* q = (const uint16_t*)d_in[0];
  const uint16_t* k = (const uint16_t*)d_in[1];
  const uint16_t* v = (const uint16_t*)d_in[2];
  const uint16_t* e = (const uint16_t*)d_in[3];
  const uint16_t* f = (const uint16_t*)d_in[4];
  float* ps = (float*)((char*)d_ws + WS_PS);
  float* po = (float*)((char*)d_ws + WS_PO);
  kphase_kernel<<<BH * S1, 64 * NWAVES, 0, stream>>>(q, k, e, ps);
  vphase_kernel<<<BH * S2, 64 * NWAVES, 0, stream>>>(v, f, ps, po);
  combine_kernel<<<BH, DH, 0, stream>>>(po, (__hip_bfloat16*)d_out);
}
```

```cpp
#include <hip/hip_runtime.h>
#include <hip/hip_bf16.h>
#include <stdint.h>

namespace {

constexpr int BH = 64;
constexpr int SKV = 8192;
constexpr int DH = 128;
constexpr int RR = 64;
constexpr float SCALE = 0.08838834764831845f;

#define KP_S1 32
#define KP_S2 32
constexpr int S1 = KP_S1;
constexpr int S2 = KP_S2;
constexpr int NWAVES = 4;

constexpr size_t WS_PS = 0;
constexpr size_t WS_PO = (size_t)1 << 20;
constexpr size_t WS_END = WS_PO + (size_t)BH * S2 * DH * 4;
static_assert((size_t)BH * S1 * RR * 4 <= WS_PO, "ws layout");

typedef __attribute__((ext_vector_type(4))) unsigned u32x4;
typedef __attribute__((ext_vector_type(4))) float f32x4;
typedef __attribute__((ext_vector_type(2))) float f32x2;
typedef __attribute__((ext_vector_type(2))) __bf16 bf16x2;

__device__ __forceinline__ float dot2(unsigned a, unsigned b, float acc) {
  return __builtin_amdgcn_fdot2_f32_bf16(__builtin_bit_cast(bf16x2, a), __builtin_bit_cast(bf16x2, b), acc, false);
}
template <int CTRL> __device__ __forceinline__ float dpp(float x) {
  return __builtin_bit_cast(float, __builtin_amdgcn_mov_dpp(__builtin_bit_cast(int, x), CTRL, 0xf, 0xf, true));
}
constexpr int DPP_XOR1 = 0xB1, DPP_XOR2 = 0x4E, DPP_XOR7 = 0x141, DPP_ROR8 = 0x128, DPP_ROR4 = 0x124,
              DPP_ROR2 = 0x122, DPP_ROR1 = 0x121;

__device__ __forceinline__ float bf_lo(unsigned u) { return __builtin_bit_cast(float, u << 16); }
__device__ __forceinline__ float bf_hi(unsigned u) { return __builtin_bit_cast(float, u & 0xffff0000u); }

__device__ __forceinline__ void wave_lds_sync() {
  __builtin_amdgcn_fence(__ATOMIC_RELEASE, "wavefront");
  __builtin_amdgcn_wave_barrier();
  __builtin_amdgcn_fence(__ATOMIC_ACQUIRE, "wavefront");
}
__device__ __forceinline__ void sched_fence() { __builtin_amdgcn_sched_barrier(0); }

__device__ __forceinline__ float swap16_add(float a, float b) {
  auto s = __builtin_amdgcn_permlane16_swap(__float_as_uint(a), __float_as_uint(b), false, false);
  return __uint_as_float(s[0]) + __uint_as_float(s[1]);
}
__device__ __forceinline__ float swap32_add(float a, float b) {
  auto s = __builtin_amdgcn_permlane32_swap(__float_as_uint(a), __float_as_uint(b), false, false);
  return __uint_as_float(s[0]) + __uint_as_float(s[1]);
}
__device__ __forceinline__ float swap16_max(float a) {
  auto s = __builtin_amdgcn_permlane16_swap(__float_as_uint(a), __float_as_uint(a), false, false);
  return fmaxf(__uint_as_float(s[0]), __uint_as_float(s[1]));
}
__device__ __forceinline__ float swap32_max(float a) {
  auto s = __builtin_amdgcn_permlane32_swap(__float_as_uint(a), __float_as_uint(a), false, false);
  return fmaxf(__uint_as_float(s[0]), __uint_as_float(s[1]));
}
__device__ __forceinline__ float wave_max(float x) {
  x = fmaxf(x, dpp<DPP_ROR8>(x)); x = fmaxf(x, dpp<DPP_ROR4>(x)); x = fmaxf(x, dpp<DPP_ROR2>(x)); x = fmaxf(x, dpp<DPP_ROR1>(x));
  x = swap16_max(x); x = swap32_max(x);
  return x;
}
__device__ __forceinline__ float wave_sum(float x) {
  x += dpp<DPP_ROR8>(x); x += dpp<DPP_ROR4>(x); x += dpp<DPP_ROR2>(x); x += dpp<DPP_ROR1>(x);
  x = swap16_add(x, x); x = swap32_add(x, x);
  return x;
}

__global__ void __launch_bounds__(256, 4) kphase_kernel(const uint16_t* __restrict__ q, const uint16_t* __restrict__ k,
                                                        const uint16_t* __restrict__ e, float* __restrict__ ps) {
  constexpr int ROWS_WG = SKV / S1, ROWS_WAVE = ROWS_WG / NWAVES;
  static_assert(ROWS_WAVE == 64, "one 64-row block per wave");
  __shared__ __attribute__((aligned(16))) float s_lds[NWAVES][64];
  __shared__ __attribute__((aligned(16))) float red[NWAVES][64];
  const int bh = blockIdx.x / S1, split = blockIdx.x % S1;
  const int tid = threadIdx.x, lane = tid & 63, wave = tid >> 6;
  const int g = lane >> 4, c = lane & 15, rs = lane >> 3, no = lane & 7;
  const int n0 = split * ROWS_WG + wave * ROWS_WAVE;
  const uint16_t* kb = k + ((size_t)bh * SKV + n0 + g) * DH + c * 8;
  const uint16_t* eb = e + (size_t)rs * SKV + n0 + no * 8;
  const u32x4 qv = *reinterpret_cast<const u32x4*>(q + bh * DH + c * 8);
  u32x4 kv[16];
#pragma unroll
  for (int i = 0; i < 16; ++i) kv[i] = __builtin_nontemporal_load(reinterpret_cast<const u32x4*>(kb + (size_t)(4 * i) * DH));
  u32x4 ev[8];
#pragma unroll
  for (int j = 0; j < 8; ++j) ev[j] = *reinterpret_cast<const u32x4*>(eb + (size_t)j * 8 * SKV);
  sched_fence();
  float x[16];
#pragma unroll
  for (int i = 0; i < 16; ++i)
    x[i] = dot2(kv[i].w, qv.w, dot2(kv[i].z, qv.z, dot2(kv[i].y, qv.y, dot2(kv[i].x, qv.x, 0.f))));
#define TR_STEP(ARR, HALF, CTRL, IDX, BIT)                                     \
  _Pragma("unroll") for (int h = 0; h < (HALF); ++h) {                         \
    const float lo_ = ARR[h], hi_ = ARR[h + (HALF)];                           \
    const float t1_ = lo_ + dpp<CTRL>(lo_), t2_ = hi_ + dpp<CTRL>(hi_);        \
    ARR[h] = ((IDX) & (BIT)) ? t2_ : t1_;                                      \
  }
  TR_STEP(x, 8, DPP_ROR8, c, 8) TR_STEP(x, 4, DPP_XOR7, c, 4) TR_STEP(x, 2, DPP_XOR2, c, 2) TR_STEP(x, 1, DPP_XOR1, c, 1)
  s_lds[wave][4 * c + g] = x[0];
  wave_lds_sync();
  const f32x4 s0 = *reinterpret_cast<const f32x4*>(&s_lds[wave][8 * no]);
  const f32x4 s1 = *reinterpret_cast<const f32x4*>(&s_lds[wave][8 * no + 4]);
  float acc[8];
#pragma unroll
  for (int j = 0; j < 8; ++j) {
    float a = bf_lo(ev[j].x) * s0.x;
    a = fmaf(bf_hi(ev[j].x), s0.y, a);
    a = fmaf(bf_lo(ev[j].y), s0.z, a); a = fmaf(bf_hi(ev[j].y), s0.w, a);
    a = fmaf(bf_lo(ev[j].z), s1.x, a); a = fmaf(bf_hi(ev[j].z), s1.y, a);
    a = fmaf(bf_lo(ev[j].w), s1.z, a); a = fmaf(bf_hi(ev[j].w), s1.w, a);
    acc[j] = a;
  }
  TR_STEP(acc, 4, DPP_XOR7, lane, 4) TR_STEP(acc, 2, DPP_XOR2, lane, 2) TR_STEP(acc, 1, DPP_XOR1, lane, 1)
  red[wave][8 * no + rs] = acc[0];
  __syncthreads();
  if (tid < RR) ps[((size_t)bh * S1 + split) * RR + tid] = (red[0][tid] + red[1][tid]) + (red[2][tid] + red[3][tid]);
}

__global__ void __launch_bounds__(256, 4) vphase_kernel(const uint16_t* __restrict__ v, const uint16_t* __restrict__ f,
                                                        const float* __restrict__ ps, float* __restrict__ po) {
  constexpr int ROWS_WG = SKV / S2, ROWS_WAVE = ROWS_WG / NWAVES;
  static_assert(ROWS_WAVE == 64, "one 64-row block per wave");
  static_assert(S1 % NWAVES == 0, "score partials split evenly over the waves");
  constexpr int SPW = S1 / NWAVES;
  __shared__ __attribute__((aligned(16))) float part_lds[NWAVES][64];
  __shared__ __attribute__((aligned(16))) float p_lds[NWAVES][64];
  __shared__ __attribute__((aligned(16))) float w_lds[NWAVES][64];
  __shared__ __attribute__((aligned(16))) float red[NWAVES][DH];
  const int bh = blockIdx.x / S2, split = blockIdx.x % S2;
  const int tid = threadIdx.x, lane = tid & 63, wave = tid >> 6;
  const int g = lane >> 4, c = lane & 15, rs = lane >> 3, no = lane & 7;
  const int n0 = split * ROWS_WG + wave * ROWS_WAVE;
  const uint16_t* vb = v + ((size_t)bh * SKV + n0 + g) * DH + c * 8;
  const uint16_t* fb = f + (size_t)rs * SKV + n0 + no * 8;

  float pp[SPW];
#pragma unroll
  for (int s = 0; s < SPW; ++s) pp[s] = ps[((size_t)bh * S1 + wave * SPW + s) * RR + lane];
  u32x4 fv[8];
#pragma unroll
  for (int j = 0; j < 8; ++j) fv[j] = *reinterpret_cast<const u32x4*>(fb + (size_t)j * 8 * SKV);
  u32x4 vv[16];
#pragma unroll
  for (int i = 0; i < 16; ++i) vv[i] = __builtin_nontemporal_load(reinterpret_cast<const u32x4*>(vb + (size_t)(4 * i) * DH));
  sched_fence();

  float part = 0.f;
#pragma unroll
  for (int s = 0; s < SPW; ++s) part += pp[s];
  part_lds[wave][lane] = part;
  __syncthreads();
  const float sc = ((part_lds[0][lane] + part_lds[1][lane]) + (part_lds[2][lane] + part_lds[3][lane])) * SCALE;
  const float m = wave_max(sc);
  const float ex = __builtin_amdgcn_exp2f((sc - m) * 1.4426950408889634f);
  const float tot = wave_sum(ex);
  p_lds[wave][lane] = ex / tot;
  wave_lds_sync();
  float preg[8];
#pragma unroll
  for (int j = 0; j < 8; ++j) preg[j] = p_lds[wave][8 * j + rs];

  float wp[8];
#pragma unroll
  for (int t = 0; t < 8; ++t) wp[t] = 0.f;
#pragma unroll
  for (int j = 0; j < 8; ++j) {
    wp[0] = fmaf(bf_lo(fv[j].x), preg[j], wp[0]); wp[1] = fmaf(bf_hi(fv[j].x), preg[j], wp[1]);
    wp[2] = fmaf(bf_lo(fv[j].y), preg[j], wp[2]); wp[3] = fmaf(bf_hi(fv[j].y), preg[j], wp[3]);
    wp[4] = fmaf(bf_lo(fv[j].z), preg[j], wp[4]); wp[5] = fmaf(bf_hi(fv[j].z), preg[j], wp[5]);
    wp[6] = fmaf(bf_lo(fv[j].w), preg[j], wp[6]); wp[7] = fmaf(bf_hi(fv[j].w), preg[j], wp[7]);
  }
  TR_STEP(wp, 4, DPP_ROR8, lane, 8)
  const float z0 = swap16_add(wp[0], wp[2]), z1 = swap16_add(wp[1], wp[3]);
  const float wval = swap32_add(z0, z1);
  const int tsel = ((lane >> 5) & 1) + 2 * ((lane >> 4) & 1) + 4 * ((lane >> 3) & 1);
  w_lds[wave][(tsel & 3) * 16 + 2 * no + (tsel >> 2)] = wval;
  wave_lds_sync();
  f32x4 wr[4];
#pragma unroll
  for (int u = 0; u < 4; ++u) wr[u] = *reinterpret_cast<const f32x4*>(&w_lds[wave][g * 16 + 4 * u]);

  float acc[8];
#pragma unroll
  for (int t = 0; t < 8; ++t) acc[t] = 0.f;
#pragma unroll
  for (int i = 0; i < 16; ++i) {
    const float wv = wr[i >> 2][i & 3];
    acc[0] = fmaf(bf_lo(vv[i].x), wv, acc[0]); acc[1] = fmaf(bf_hi(vv[i].x), wv, acc[1]);
    acc[2] = fmaf(bf_lo(vv[i].y), wv, acc[2]); acc[3] = fmaf(bf_hi(vv[i].y), wv, acc[3]);
    acc[4] = fmaf(bf_lo(vv[i].z), wv, acc[4]); acc[5] = fmaf(bf_hi(vv[i].z), wv, acc[5]);
    acc[6] = fmaf(bf_lo(vv[i].w), wv, acc[6]); acc[7] = fmaf(bf_hi(vv[i].w), wv, acc[7]);
  }
#undef TR_STEP
  const float y0 = swap16_add(acc[0], acc[4]), y1 = swap16_add(acc[1], acc[5]);
  const float y2 = swap16_add(acc[2], acc[6]), y3 = swap16_add(acc[3], acc[7]);
  const float o0 = swap32_add(y0, y2), o1 = swap32_add(y1, y3);
  *reinterpret_cast<f32x2*>(&red[wave][8 * c + 4 * ((lane >> 4) & 1) + 2 * (lane >> 5)]) = f32x2{o0, o1};
  __syncthreads();
  if (tid < DH) po[((size_t)bh * S2 + split) * DH + tid] = (red[0][tid] + red[1][tid]) + (red[2][tid] + red[3][tid]);
}

__global__ void __launch_bounds__(DH) combine_kernel(const float* __restrict__ po, __hip_bfloat16* __restrict__ out) {
  const int bh = blockIdx.x, d = threadIdx.x;
  float t[S2];
#pragma unroll
  for (int i = 0; i < S2; ++i) t[i] = po[((size_t)bh * S2 + i) * DH + d];
  float s = 0.f;
#pragma unroll
  for (int i = 0; i < S2; ++i) s += t[i];
  out[bh * DH + d] = __float2bfloat16(s);
}

}

extern "C" void kernel_launch(void* const* d_in, const int* in_sizes, int n_in,
                              void* d_out, int out_size, void* d_ws, size_t ws_size,
                              hipStream_t stream) {
  if (n_in != 5 || in_sizes[0] != BH * DH || in_sizes[1] != BH * SKV * DH || in_sizes[2] != BH * SKV * DH ||
      in_sizes[3] != RR * SKV || in_sizes[4] != RR * SKV || out_size != BH * DH || ws_size < WS_END)
    return;
  const uint16_t* q = (const uint16_t*)d_in[0];
  const uint16_t* k = (const uint16_t*)d_in[1];
  const uint16_t* v = (const uint16_t*)d_in[2];
  const uint16_t* e = (const uint16_t*)d_in[3];
  const uint16_t* f = (const uint16_t*)d_in[4];
  float* ps = (float*)((char*)d_ws + WS_PS);
  float* po = (float*)((char*)d_ws + WS_PO);
  kphase_kernel<<<BH * S1, 64 * NWAVES, 0, stream>>>(q, k, e, ps);
  vphase_kernel<<<BH * S2, 64 * NWAVES, 0, stream>>>(v, f, ps, po);
  combine_kernel<<<BH, DH, 0, stream>>>(po, (__hip_bfloat16*)d_out);
}
```

```cpp
#include <hip/hip_runtime.h>
#include <hip/hip_bf16.h>
#include <stdint.h>

namespace {

constexpr int BH = 64;
constexpr int SKV = 8192;
constexpr int DH = 128;
constexpr int RR = 64;
constexpr float SCALE = 0.08838834764831845f;

#define KP_NB1 2
#define KP_NB2 2
#define KP_WPS 3
constexpr int NWAVES = 4;
constexpr int NB1 = KP_NB1;
constexpr int NB2 = KP_NB2;
constexpr int S1 = SKV / (64 * NWAVES * NB1);
constexpr int S2 = SKV / (64 * NWAVES * NB2);

constexpr size_t WS_PS = 0;
constexpr size_t WS_PO = (size_t)1 << 20;
constexpr size_t WS_END = WS_PO + (size_t)BH * S2 * DH * 4;
static_assert((size_t)BH * S1 * RR * 4 <= WS_PO, "ws layout");

typedef __attribute__((ext_vector_type(4))) unsigned u32x4;
typedef __attribute__((ext_vector_type(4))) float f32x4;
typedef __attribute__((ext_vector_type(2))) float f32x2;
typedef __attribute__((ext_vector_type(2))) __bf16 bf16x2;

__device__ __forceinline__ float dot2(unsigned a, unsigned b, float acc) {
  return __builtin_amdgcn_fdot2_f32_bf16(__builtin_bit_cast(bf16x2, a), __builtin_bit_cast(bf16x2, b), acc, false);
}
template <int CTRL> __device__ __forceinline__ float dpp(float x) {
  return __builtin_bit_cast(float, __builtin_amdgcn_mov_dpp(__builtin_bit_cast(int, x), CTRL, 0xf, 0xf, true));
}
constexpr int DPP_XOR1 = 0xB1, DPP_XOR2 = 0x4E, DPP_XOR7 = 0x141, DPP_ROR8 = 0x128, DPP_ROR4 = 0x124,
              DPP_ROR2 = 0x122, DPP_ROR1 = 0x121;

__device__ __forceinline__ float bf_lo(unsigned u) { return __builtin_bit_cast(float, u << 16); }
__device__ __forceinline__ float bf_hi(unsigned u) { return __builtin_bit_cast(float, u & 0xffff0000u); }
__device__ __forceinline__ u32x4 ld_stream(const uint16_t* p) { return __builtin_nontemporal_load(reinterpret_cast<const u32x4*>(p)); }
__device__ __forceinline__ u32x4 ld_tab(const uint16_t* p) { return *reinterpret_cast<const u32x4*>(p); }

__device__ __forceinline__ void wave_lds_sync() {
  __builtin_amdgcn_fence(__ATOMIC_RELEASE, "wavefront");
  __builtin_amdgcn_wave_barrier();
  __builtin_amdgcn_fence(__ATOMIC_ACQUIRE, "wavefront");
}
__device__ __forceinline__ void sched_fence() { __builtin_amdgcn_sched_barrier(0); }

__device__ __forceinline__ float swap16_add(float a, float b) {
  auto s = __builtin_amdgcn_permlane16_swap(__float_as_uint(a), __float_as_uint(b), false, false);
  return __uint_as_float(s[0]) + __uint_as_float(s[1]);
}
__device__ __forceinline__ float swap32_add(float a, float b) {
  auto s = __builtin_amdgcn_permlane32_swap(__float_as_uint(a), __float_as_uint(b), false, false);
  return __uint_as_float(s[0]) + __uint_as_float(s[1]);
}
__device__ __forceinline__ float swap16_max(float a) {
  auto s = __builtin_amdgcn_permlane16_swap(__float_as_uint(a), __float_as_uint(a), false, false);
  return fmaxf(__uint_as_float(s[0]), __uint_as_float(s[1]));
}
__device__ __forceinline__ float swap32_max(float a) {
  auto s = __builtin_amdgcn_permlane32_swap(__float_as_uint(a), __float_as_uint(a), false, false);
  return fmaxf(__uint_as_float(s[0]), __uint_as_float(s[1]));
}
__device__ __forceinline__ float wave_max(float x) {
  x = fmaxf(x, dpp<DPP_ROR8>(x)); x = fmaxf(x, dpp<DPP_ROR4>(x)); x = fmaxf(x, dpp<DPP_ROR2>(x)); x = fmaxf(x, dpp<DPP_ROR1>(x));
  x = swap16_max(x); x = swap32_max(x);
  return x;
}
__device__ __forceinline__ float wave_sum(float x) {
  x += dpp<DPP_ROR8>(x); x += dpp<DPP_ROR4>(x); x += dpp<DPP_ROR2>(x); x += dpp<DPP_ROR1>(x);
  x = swap16_add(x, x); x = swap32_add(x, x);
  return x;
}

#define TR_STEP(ARR, HALF, CTRL, IDX, BIT)                                     \
  _Pragma("unroll") for (int h = 0; h < (HALF); ++h) {                         \
    const float lo_ = ARR[h], hi_ = ARR[h + (HALF)];                           \
    const float t1_ = lo_ + dpp<CTRL>(lo_), t2_ = hi_ + dpp<CTRL>(hi_);        \
    ARR[h] = ((IDX) & (BIT)) ? t2_ : t1_;                                      \
  }

__global__ void __launch_bounds__(64 * NWAVES, KP_WPS) kphase_kernel(const uint16_t* __restrict__ q, const uint16_t* __restrict__ k,
                                                                const uint16_t* __restrict__ e, float* __restrict__ ps) {
  constexpr int ROWS_WAVE = 64 * NB1, ROWS_WG = ROWS_WAVE * NWAVES;
  __shared__ __attribute__((aligned(16))) float s_lds[NWAVES][64];
  __shared__ __attribute__((aligned(16))) float red[NWAVES][64];
  const int bh = blockIdx.x / S1, split = blockIdx.x % S1;
  const int tid = threadIdx.x, lane = tid & 63, wave = tid >> 6;
  const int g = lane >> 4, c = lane & 15, rs = lane >> 3, no = lane & 7;
  const int n0 = split * ROWS_WG + wave * ROWS_WAVE;
  const uint16_t* kb = k + ((size_t)bh * SKV + n0 + g) * DH + c * 8;
  const uint16_t* eb = e + (size_t)rs * SKV + n0 + no * 8;
  const u32x4 qv = ld_tab(q + bh * DH + c * 8);
  u32x4 kv[16];
#pragma unroll
  for (int i = 0; i < 16; ++i) kv[i] = ld_stream(kb + (size_t)(4 * i) * DH);
  u32x4 ev[8];
#pragma unroll
  for (int j = 0; j < 8; ++j) ev[j] = ld_tab(eb + (size_t)j * 8 * SKV);
  sched_fence();
  float acc[8];
#pragma unroll
  for (int j = 0; j < 8; ++j) acc[j] = 0.f;
#pragma unroll
  for (int blk = 0; blk < NB1; ++blk) {
    float x[16];
#pragma unroll
    for (int i4 = 0; i4 < 16; i4 += 4) {
#pragma unroll
      for (int i = i4; i < i4 + 4; ++i)
        x[i] = dot2(kv[i].w, qv.w, dot2(kv[i].z, qv.z, dot2(kv[i].y, qv.y, dot2(kv[i].x, qv.x, 0.f))));
      if (blk + 1 < NB1) {
        sched_fence();
#pragma unroll
        for (int i = i4; i < i4 + 4; ++i) kv[i] = ld_stream(kb + (size_t)((blk + 1) * 64 + 4 * i) * DH);
        sched_fence();
      }
    }
    TR_STEP(x, 8, DPP_ROR8, c, 8) TR_STEP(x, 4, DPP_XOR7, c, 4) TR_STEP(x, 2, DPP_XOR2, c, 2) TR_STEP(x, 1, DPP_XOR1, c, 1)
    s_lds[wave][4 * c + g] = x[0];
    wave_lds_sync();
    const f32x4 s0 = *reinterpret_cast<const f32x4*>(&s_lds[wave][8 * no]);
    const f32x4 s1 = *reinterpret_cast<const f32x4*>(&s_lds[wave][8 * no + 4]);
    wave_lds_sync();
#pragma unroll
    for (int j = 0; j < 8; ++j) {
      float a = acc[j];
      a = fmaf(bf_lo(ev[j].x), s0.x, a); a = fmaf(bf_hi(ev[j].x), s0.y, a);
      a = fmaf(bf_lo(ev[j].y), s0.z, a); a = fmaf(bf_hi(ev[j].y), s0.w, a);
      a = fmaf(bf_lo(ev[j].z), s1.x, a); a = fmaf(bf_hi(ev[j].z), s1.y, a);
      a = fmaf(bf_lo(ev[j].w), s1.z, a); a = fmaf(bf_hi(ev[j].w), s1.w, a);
      acc[j] = a;
    }
    if (blk + 1 < NB1) {
      sched_fence();
#pragma unroll
      for (int j = 0; j < 8; ++j) ev[j] = ld_tab(eb + (size_t)j * 8 * SKV + (blk + 1) * 64);
      sched_fence();
    }
  }
  TR_STEP(acc, 4, DPP_XOR7, lane, 4) TR_STEP(acc, 2, DPP_XOR2, lane, 2) TR_STEP(acc, 1, DPP_XOR1, lane, 1)
  red[wave][8 * no + rs] = acc[0];
  __syncthreads();
  if (tid < RR) {
    float s = 0.f;
#pragma unroll
    for (int w = 0; w < NWAVES; ++w) s += red[w][tid];
    ps[((size_t)bh * S1 + split) * RR + tid] = s;
  }
}

__global__ void __launch_bounds__(64 * NWAVES, KP_WPS) vphase_kernel(const uint16_t* __restrict__ v, const uint16_t* __restrict__ f,
                                                                const float* __restrict__ ps, float* __restrict__ po) {
  constexpr int ROWS_WAVE = 64 * NB2, ROWS_WG = ROWS_WAVE * NWAVES;
  static_assert(S1 % NWAVES == 0, "score partials split evenly over the waves");
  constexpr int SPW = S1 / NWAVES;
  __shared__ __attribute__((aligned(16))) float part_lds[NWAVES][64];
  __shared__ __attribute__((aligned(16))) float p_lds[NWAVES][64];
  __shared__ __attribute__((aligned(16))) float w_lds[NWAVES][64];
  __shared__ __attribute__((aligned(16))) float red[NWAVES][DH];
  const int bh = blockIdx.x / S2, split = blockIdx.x % S2;
  const int tid = threadIdx.x, lane = tid & 63, wave = tid >> 6;
  const int g = lane >> 4, c = lane & 15, rs = lane >> 3, no = lane & 7;
  const int n0 = split * ROWS_WG + wave * ROWS_WAVE;
  const uint16_t* vb = v + ((size_t)bh * SKV + n0 + g) * DH + c * 8;
  const uint16_t* fb = f + (size_t)rs * SKV + n0 + no * 8;

  float pp[SPW];
#pragma unroll
  for (int s = 0; s < SPW; ++s) pp[s] = ps[((size_t)bh * S1 + wave * SPW + s) * RR + lane];
  u32x4 fv[8];
#pragma unroll
  for (int j = 0; j < 8; ++j) fv[j] = ld_tab(fb + (size_t)j * 8 * SKV);
  u32x4 vv[16];
#pragma unroll
  for (int i = 0; i < 16; ++i) vv[i] = ld_stream(vb + (size_t)(4 * i) * DH);
  sched_fence();

  float part = 0.f;
#pragma unroll
  for (int s = 0; s < SPW; ++s) part += pp[s];
  part_lds[wave][lane] = part;
  __syncthreads();
  float sraw = 0.f;
#pragma unroll
  for (int w = 0; w < NWAVES; ++w) sraw += part_lds[w][lane];
  const float sc = sraw * SCALE;
  const float m = wave_max(sc);
  const float ex = __builtin_amdgcn_exp2f((sc - m) * 1.4426950408889634f);
  const float tot = wave_sum(ex);
  p_lds[wave][lane] = ex / tot;
  wave_lds_sync();
  float preg[8];
#pragma unroll
  for (int j = 0; j < 8; ++j) preg[j] = p_lds[wave][8 * j + rs];

  const int tsel = ((lane >> 5) & 1) + 2 * ((lane >> 4) & 1) + 4 * ((lane >> 3) & 1);
  const int wpos = (tsel & 3) * 16 + 2 * no + (tsel >> 2);

  float acc[8];
#pragma unroll
  for (int t = 0; t < 8; ++t) acc[t] = 0.f;
#pragma unroll
  for (int blk = 0; blk < NB2; ++blk) {
    float wp[8];
#pragma unroll
    for (int t = 0; t < 8; ++t) wp[t] = 0.f;
#pragma unroll
    for (int j = 0; j < 8; ++j) {
      wp[0] = fmaf(bf_lo(fv[j].x), preg[j], wp[0]); wp[1] = fmaf(bf_hi(fv[j].x), preg[j], wp[1]);
      wp[2] = fmaf(bf_lo(fv[j].y), preg[j], wp[2]); wp[3] = fmaf(bf_hi(fv[j].y), preg[j], wp[3]);
      wp[4] = fmaf(bf_lo(fv[j].z), preg[j], wp[4]); wp[5] = fmaf(bf_hi(fv[j].z), preg[j], wp[5]);
      wp[6] = fmaf(bf_lo(fv[j].w), preg[j], wp[6]); wp[7] = fmaf(bf_hi(fv[j].w), preg[j], wp[7]);
    }
    if (blk + 1 < NB2) {
      sched_fence();
#pragma unroll
      for (int j = 0; j < 8; ++j) fv[j] = ld_tab(fb + (size_t)j * 8 * SKV + (blk + 1) * 64);
      sched_fence();
    }
    TR_STEP(wp, 4, DPP_ROR8, lane, 8)
    const float z0 = swap16_add(wp[0], wp[2]), z1 = swap16_add(wp[1], wp[3]);
    const float wval = swap32_add(z0, z1);
    w_lds[wave][wpos] = wval;
    wave_lds_sync();
    f32x4 wr[4];
#pragma unroll
    for (int u = 0; u < 4; ++u) wr[u] = *reinterpret_cast<const f32x4*>(&w_lds[wave][g * 16 + 4 * u]);
    wave_lds_sync();
#pragma unroll
    for (int i4 = 0; i4 < 16; i4 += 4) {
#pragma unroll
      for (int i = i4; i < i4 + 4; ++i) {
        const float wv = wr[i >> 2][i & 3];
        acc[0] = fmaf(bf_lo(vv[i].x), wv, acc[0]); acc[1] = fmaf(bf_hi(vv[i].x), wv, acc[1]);
        acc[2] = fmaf(bf_lo(vv[i].y), wv, acc[2]); acc[3] = fmaf(bf_hi(vv[i].y), wv, acc[3]);
        acc[4] = fmaf(bf_lo(vv[i].z), wv, acc[4]); acc[5] = fmaf(bf_hi(vv[i].z), wv, acc[5]);
        acc[6] = fmaf(bf_lo(vv[i].w), wv, acc[6]); acc[7] = fmaf(bf_hi(vv[i].w), wv, acc[7]);
      }
      if (blk + 1 < NB2) {
        sched_fence();
#pragma unroll
        for (int i = i4; i < i4 + 4; ++i) vv[i] = ld_stream(vb + (size_t)((blk + 1) * 64 + 4 * i) * DH);
        sched_fence();
      }
    }
  }
  const float y0 = swap16_add(acc[0], acc[4]), y1 = swap16_add(acc[1], acc[5]);
  const float y2 = swap16_add(acc[2], acc[6]), y3 = swap16_add(acc[3], acc[7]);
  const float o0 = swap32_add(y0, y2), o1 = swap32_add(y1, y3);
  *reinterpret_cast<f32x2*>(&red[wave][8 * c + 4 * ((lane >> 4) & 1) + 2 * (lane >> 5)]) = f32x2{o0, o1};
  __syncthreads();
  if (tid < DH) {
    float s = 0.f;
#pragma unroll
    for (int w = 0; w < NWAVES; ++w) s += red[w][tid];
    po[((size_t)bh * S2 + split) * DH + tid] = s;
  }
}
#undef TR_STEP

__global__ void __launch_bounds__(DH) combine_kernel(const float* __restrict__ po, __hip_bfloat16* __restrict__ out) {
  const int bh = blockIdx.x, d = threadIdx.x;
  float t[S2];
#pragma unroll
  for (int i = 0; i < S2; ++i) t[i] = po[((size_t)bh * S2 + i) * DH + d];
  float s = 0.f;
#pragma unroll
  for (int i = 0; i < S2; ++i) s += t[i];
  out[bh * DH + d] = __float2bfloat16(s);
}

}

extern "C" void kernel_launch(void* const* d_in, const int* in_sizes, int n_in,
                              void* d_out, int out_size, void* d_ws, size_t ws_size,
                              hipStream_t stream) {
  if (n_in != 5 || in_sizes[0] != BH * DH || in_sizes[1] != BH * SKV * DH || in_sizes[2] != BH * SKV * DH ||
      in_sizes[3] != RR * SKV || in_sizes[4] != RR * SKV || out_size != BH * DH || ws_size < WS_END)
    return;
  const uint16_t* q = (const uint16_t*)d_in[0];
  const uint16_t* k = (const uint16_t*)d_in[1];
  const uint16_t* v = (const uint16_t*)d_in[2];
  const uint16_t* e = (const uint16_t*)d_in[3];
  const uint16_t* f = (const uint16_t*)d_in[4];
  float* ps = (float*)((char*)d_ws + WS_PS);
  float* po = (float*)((char*)d_ws + WS_PO);
  kphase_kernel<<<BH * S1, 64 * NWAVES, 0, stream>>>(q, k, e, ps);
  vphase_kernel<<<BH * S2, 64 * NWAVES, 0, stream>>>(v, f, ps, po);
  combine_kernel<<<BH, DH, 0, stream>>>(po, (__hip_bfloat16*)d_out);
}
```

```cpp
#include <hip/hip_runtime.h>
#include <hip/hip_bf16.h>
#include <stdint.h>

namespace {

constexpr int BH = 64;
constexpr int SKV = 8192;
constexpr int DH = 128;
constexpr int RR = 64;
constexpr float SCALE = 0.08838834764831845f;

#define KP_NW1 4
#define KP_NW2 4
#define KP_NB1 2
#define KP_NB2 2
constexpr int NW1 = KP_NW1, NW2 = KP_NW2;
constexpr int NB1 = KP_NB1, NB2 = KP_NB2;
constexpr int S1 = SKV / (64 * NW1 * NB1);
constexpr int S2 = SKV / (64 * NW2 * NB2);

constexpr size_t WS_PS = 0;
constexpr size_t WS_PO = (size_t)2 << 20;
constexpr size_t WS_END = WS_PO + (size_t)BH * S2 * DH * 4;
static_assert((size_t)BH * S1 * RR * 4 <= WS_PO, "ws layout");

typedef __attribute__((ext_vector_type(4))) unsigned u32x4;
typedef __attribute__((ext_vector_type(4))) float f32x4;
typedef __attribute__((ext_vector_type(2))) float f32x2;
typedef __attribute__((ext_vector_type(2))) __bf16 bf16x2;

__device__ __forceinline__ float dot2(unsigned a, unsigned b, float acc) {
  return __builtin_amdgcn_fdot2_f32_bf16(__builtin_bit_cast(bf16x2, a), __builtin_bit_cast(bf16x2, b), acc, false);
}
template <int CTRL> __device__ __forceinline__ float dpp(float x) {
  return __builtin_bit_cast(float, __builtin_amdgcn_mov_dpp(__builtin_bit_cast(int, x), CTRL, 0xf, 0xf, true));
}
constexpr int DPP_XOR1 = 0xB1, DPP_XOR2 = 0x4E, DPP_XOR7 = 0x141, DPP_ROR8 = 0x128, DPP_ROR4 = 0x124,
              DPP_ROR2 = 0x122, DPP_ROR1 = 0x121;

__device__ __forceinline__ float bf_lo(unsigned u) { return __builtin_bit_cast(float, u << 16); }
__device__ __forceinline__ float bf_hi(unsigned u) { return __builtin_bit_cast(float, u & 0xffff0000u); }
__device__ __forceinline__ f32x2 bf_pair(unsigned u) { return f32x2{bf_lo(u), bf_hi(u)}; }
__device__ __forceinline__ f32x2 fma2(f32x2 a, f32x2 b, f32x2 c) { return __builtin_elementwise_fma(a, b, c); }
__device__ __forceinline__ u32x4 ld_stream(const uint16_t* p) { return __builtin_nontemporal_load(reinterpret_cast<const u32x4*>(p)); }
__device__ __forceinline__ u32x4 ld_tab(const uint16_t* p) { return *reinterpret_cast<const u32x4*>(p); }

__device__ __forceinline__ void wave_lds_sync() {
  __builtin_amdgcn_fence(__ATOMIC_RELEASE, "wavefront");
  __builtin_amdgcn_wave_barrier();
  __builtin_amdgcn_fence(__ATOMIC_ACQUIRE, "wavefront");
}
__device__ __forceinline__ void sched_fence() { __builtin_amdgcn_sched_barrier(0); }

__device__ __forceinline__ float swap16_add(float a, float b) {
  auto s = __builtin_amdgcn_permlane16_swap(__float_as_uint(a), __float_as_uint(b), false, false);
  return __uint_as_float(s[0]) + __uint_as_float(s[1]);
}
__device__ __forceinline__ float swap32_add(float a, float b) {
  auto s = __builtin_amdgcn_permlane32_swap(__float_as_uint(a), __float_as_uint(b), false, false);
  return __uint_as_float(s[0]) + __uint_as_float(s[1]);
}
__device__ __forceinline__ float swap16_max(float a) {
  auto s = __builtin_amdgcn_permlane16_swap(__float_as_uint(a), __float_as_uint(a), false, false);
  return fmaxf(__uint_as_float(s[0]), __uint_as_float(s[1]));
}
__device__ __forceinline__ float swap32_max(float a) {
  auto s = __builtin_amdgcn_permlane32_swap(__float_as_uint(a), __float_as_uint(a), false, false);
  return fmaxf(__uint_as_float(s[0]), __uint_as_float(s[1]));
}
__device__ __forceinline__ float wave_max(float x) {
  x = fmaxf(x, dpp<DPP_ROR8>(x)); x = fmaxf(x, dpp<DPP_ROR4>(x)); x = fmaxf(x, dpp<DPP_ROR2>(x)); x = fmaxf(x, dpp<DPP_ROR1>(x));
  x = swap16_max(x); x = swap32_max(x);
  return x;
}
__device__ __forceinline__ float wave_sum(float x) {
  x += dpp<DPP_ROR8>(x); x += dpp<DPP_ROR4>(x); x += dpp<DPP_ROR2>(x); x += dpp<DPP_ROR1>(x);
  x = swap16_add(x, x); x = swap32_add(x, x);
  return x;
}

#define TR_STEP(ARR, HALF, CTRL, IDX, BIT)                                     \
  _Pragma("unroll") for (int h = 0; h < (HALF); ++h) {                         \
    const float lo_ = ARR[h], hi_ = ARR[h + (HALF)];                           \
    const float t1_ = lo_ + dpp<CTRL>(lo_), t2_ = hi_ + dpp<CTRL>(hi_);        \
    ARR[h] = ((IDX) & (BIT)) ? t2_ : t1_;                                      \
  }

template <int S> __device__ __forceinline__ void map_block(int b, int& bh, int& split) { bh = b / S; split = b % S; }

__global__ void __launch_bounds__(64 * NW1, 3) kphase_kernel(const uint16_t* __restrict__ q, const uint16_t* __restrict__ k,
                                                             const uint16_t* __restrict__ e, float* __restrict__ ps) {
  constexpr int ROWS_WAVE = 64 * NB1, ROWS_WG = ROWS_WAVE * NW1;
  __shared__ __attribute__((aligned(16))) float s_lds[NW1][64];
  __shared__ __attribute__((aligned(16))) float red[NW1][64];
  int bh, split; map_block<S1>(blockIdx.x, bh, split);
  const int tid = threadIdx.x, lane = tid & 63, wave = tid >> 6;
  const int g = lane >> 4, c = lane & 15, rs = lane >> 3, no = lane & 7;
  const int n0 = split * ROWS_WG + wave * ROWS_WAVE;
  const uint16_t* kb = k + ((size_t)bh * SKV + n0 + g) * DH + c * 8;
  const uint16_t* eb = e + (size_t)rs * SKV + n0 + no * 8;
  const u32x4 qv = ld_tab(q + bh * DH + c * 8);
  u32x4 kv[16];
#pragma unroll
  for (int i = 0; i < 16; ++i) kv[i] = ld_stream(kb + (size_t)(4 * i) * DH);
  u32x4 ev[8];
#pragma unroll
  for (int j = 0; j < 8; ++j) ev[j] = ld_tab(eb + (size_t)j * 8 * SKV);
  sched_fence();
  f32x2 acc2[8];
#pragma unroll
  for (int j = 0; j < 8; ++j) acc2[j] = f32x2{0.f, 0.f};
#pragma unroll
  for (int blk = 0; blk < NB1; ++blk) {
    float x[16];
#pragma unroll
    for (int i4 = 0; i4 < 16; i4 += 4) {
#pragma unroll
      for (int i = i4; i < i4 + 4; ++i)
        x[i] = dot2(kv[i].w, qv.w, dot2(kv[i].z, qv.z, dot2(kv[i].y, qv.y, dot2(kv[i].x, qv.x, 0.f))));
      if (blk + 1 < NB1) {
        sched_fence();
#pragma unroll
        for (int i = i4; i < i4 + 4; ++i) kv[i] = ld_stream(kb + (size_t)((blk + 1) * 64 + 4 * i) * DH);
        sched_fence();
      }
    }
    TR_STEP(x, 8, DPP_ROR8, c, 8) TR_STEP(x, 4, DPP_XOR7, c, 4) TR_STEP(x, 2, DPP_XOR2, c, 2) TR_STEP(x, 1, DPP_XOR1, c, 1)
    s_lds[wave][4 * c + g] = x[0];
    wave_lds_sync();
    const f32x4 s0 = *reinterpret_cast<const f32x4*>(&s_lds[wave][8 * no]);
    const f32x4 s1 = *reinterpret_cast<const f32x4*>(&s_lds[wave][8 * no + 4]);
    wave_lds_sync();
#pragma unroll
    for (int j = 0; j < 8; ++j) {
      f32x2 a = acc2[j];
      a = fma2(bf_pair(ev[j].x), f32x2{s0.x, s0.y}, a);
      a = fma2(bf_pair(ev[j].y), f32x2{s0.z, s0.w}, a);
      a = fma2(bf_pair(ev[j].z), f32x2{s1.x, s1.y}, a);
      a = fma2(bf_pair(ev[j].w), f32x2{s1.z, s1.w}, a);
      acc2[j] = a;
    }
    if (blk + 1 < NB1) {
      sched_fence();
#pragma unroll
      for (int j = 0; j < 8; ++j) ev[j] = ld_tab(eb + (size_t)j * 8 * SKV + (blk + 1) * 64);
      sched_fence();
    }
  }
  float acc[8];
#pragma unroll
  for (int j = 0; j < 8; ++j) acc[j] = acc2[j].x + acc2[j].y;
  TR_STEP(acc, 4, DPP_XOR7, lane, 4) TR_STEP(acc, 2, DPP_XOR2, lane, 2) TR_STEP(acc, 1, DPP_XOR1, lane, 1)
  red[wave][8 * no + rs] = acc[0];
  __syncthreads();
  if (tid < RR) {
    float s = 0.f;
#pragma unroll
    for (int w = 0; w < NW1; ++w) s += red[w][tid];
    ps[((size_t)bh * S1 + split) * RR + tid] = s;
  }
}

__global__ void __launch_bounds__(64 * NW2, 3) vphase_kernel(const uint16_t* __restrict__ v, const uint16_t* __restrict__ f,
                                                             const float* __restrict__ ps, float* __restrict__ po) {
  constexpr int ROWS_WAVE = 64 * NB2, ROWS_WG = ROWS_WAVE * NW2;
  static_assert(S1 % NW2 == 0, "score partials split evenly over the waves");
  constexpr int SPW = S1 / NW2;
  __shared__ __attribute__((aligned(16))) float part_lds[NW2][64];
  __shared__ __attribute__((aligned(16))) float p_lds[NW2][64];
  __shared__ __attribute__((aligned(16))) float w_lds[NW2][64];
  __shared__ __attribute__((aligned(16))) float red[NW2][DH];
  int bh, split; map_block<S2>(blockIdx.x, bh, split);
  const int tid = threadIdx.x, lane = tid & 63, wave = tid >> 6;
  const int g = lane >> 4, c = lane & 15, rs = lane >> 3, no = lane & 7;
  const int n0 = split * ROWS_WG + wave * ROWS_WAVE;
  const uint16_t* vb = v + ((size_t)bh * SKV + n0 + g) * DH + c * 8;
  const uint16_t* fb = f + (size_t)rs * SKV + n0 + no * 8;

  float pp[SPW];
#pragma unroll
  for (int s = 0; s < SPW; ++s) pp[s] = ps[((size_t)bh * S1 + wave * SPW + s) * RR + lane];
  u32x4 fv[8];
#pragma unroll
  for (int j = 0; j < 8; ++j) fv[j] = ld_tab(fb + (size_t)j * 8 * SKV);
  u32x4 vv[16];
#pragma unroll
  for (int i = 0; i < 16; ++i) vv[i] = ld_stream(vb + (size_t)(4 * i) * DH);
  sched_fence();

  float part = 0.f;
#pragma unroll
  for (int s = 0; s < SPW; ++s) part += pp[s];
  part_lds[wave][lane] = part;
  __syncthreads();
  float sraw = 0.f;
#pragma unroll
  for (int w = 0; w < NW2; ++w) sraw += part_lds[w][lane];
  const float sc = sraw * SCALE;
  const float m = wave_max(sc);
  const float ex = __builtin_amdgcn_exp2f((sc - m) * 1.4426950408889634f);
  const float tot = wave_sum(ex);
  p_lds[wave][lane] = ex / tot;
  wave_lds_sync();
  float preg[8];
#pragma unroll
  for (int j = 0; j < 8; ++j) preg[j] = p_lds[wave][8 * j + rs];

  const int tsel = ((lane >> 5) & 1) + 2 * ((lane >> 4) & 1) + 4 * ((lane >> 3) & 1);
  const int wpos = (tsel & 3) * 16 + 2 * no + (tsel >> 2);

  f32x2 acc2[4];
#pragma unroll
  for (int t = 0; t < 4; ++t) acc2[t] = f32x2{0.f, 0.f};
#pragma unroll
  for (int blk = 0; blk < NB2; ++blk) {
    f32x2 wp2[4];
#pragma unroll
    for (int t = 0; t < 4; ++t) wp2[t] = f32x2{0.f, 0.f};
#pragma unroll
    for (int j = 0; j < 8; ++j) {
      const f32x2 pj = f32x2{preg[j], preg[j]};
      wp2[0] = fma2(bf_pair(fv[j].x), pj, wp2[0]); wp2[1] = fma2(bf_pair(fv[j].y), pj, wp2[1]);
      wp2[2] = fma2(bf_pair(fv[j].z), pj, wp2[2]); wp2[3] = fma2(bf_pair(fv[j].w), pj, wp2[3]);
    }
    float wp[8] = {wp2[0].x, wp2[0].y, wp2[1].x, wp2[1].y, wp2[2].x, wp2[2].y, wp2[3].x, wp2[3].y};
    if (blk + 1 < NB2) {
      sched_fence();
#pragma unroll
      for (int j = 0; j < 8; ++j) fv[j] = ld_tab(fb + (size_t)j * 8 * SKV + (blk + 1) * 64);
      sched_fence();
    }
    TR_STEP(wp, 4, DPP_ROR8, lane, 8)
    const float z0 = swap16_add(wp[0], wp[2]), z1 = swap16_add(wp[1], wp[3]);
    const float wval = swap32_add(z0, z1);
    w_lds[wave][wpos] = wval;
    wave_lds_sync();
    f32x4 wr[4];
#pragma unroll
    for (int u = 0; u < 4; ++u) wr[u] = *reinterpret_cast<const f32x4*>(&w_lds[wave][g * 16 + 4 * u]);
    wave_lds_sync();
#pragma unroll
    for (int i4 = 0; i4 < 16; i4 += 4) {
#pragma unroll
      for (int i = i4; i < i4 + 4; ++i) {
        const float wv = wr[i >> 2][i & 3];
        const f32x2 w2 = f32x2{wv, wv};
        acc2[0] = fma2(bf_pair(vv[i].x), w2, acc2[0]); acc2[1] = fma2(bf_pair(vv[i].y), w2, acc2[1]);
        acc2[2] = fma2(bf_pair(vv[i].z), w2, acc2[2]); acc2[3] = fma2(bf_pair(vv[i].w), w2, acc2[3]);
      }
      if (blk + 1 < NB2) {
        sched_fence();
#pragma unroll
        for (int i = i4; i < i4 + 4; ++i) vv[i] = ld_stream(vb + (size_t)((blk + 1) * 64 + 4 * i) * DH);
        sched_fence();
      }
    }
  }
  const float acc[8] = {acc2[0].x, acc2[0].y, acc2[1].x, acc2[1].y, acc2[2].x, acc2[2].y, acc2[3].x, acc2[3].y};
  const float y0 = swap16_add(acc[0], acc[4]), y1 = swap16_add(acc[1], acc[5]);
  const float y2 = swap16_add(acc[2], acc[6]), y3 = swap16_add(acc[3], acc[7]);
  const float o0 = swap32_add(y0, y2), o1 = swap32_add(y1, y3);
  *reinterpret_cast<f32x2*>(&red[wave][8 * c + 4 * ((lane >> 4) & 1) + 2 * (lane >> 5)]) = f32x2{o0, o1};
  __syncthreads();
  for (int d = tid; d < DH; d += 64 * NW2) {
    float s = 0.f;
#pragma unroll
    for (int w = 0; w < NW2; ++w) s += red[w][d];
    po[((size_t)bh * S2 + split) * DH + d] = s;
  }
}
#undef TR_STEP

__global__ void __launch_bounds__(DH) combine_kernel(const float* __restrict__ po, __hip_bfloat16* __restrict__ out) {
  const int bh = blockIdx.x, d = threadIdx.x;
  float t[S2];
#pragma unroll
  for (int i = 0; i < S2; ++i) t[i] = po[((size_t)bh * S2 + i) * DH + d];
  float s = 0.f;
#pragma unroll
  for (int i = 0; i < S2; ++i) s += t[i];
  out[bh * DH + d] = __float2bfloat16(s);
}

}

extern "C" void kernel_launch(void* const* d_in, const int* in_sizes, int n_in,
                              void* d_out, int out_size, void* d_ws, size_t ws_size,
                              hipStream_t stream) {
  if (n_in != 5 || in_sizes[0] != BH * DH || in_sizes[1] != BH * SKV * DH || in_sizes[2] != BH * SKV * DH ||
      in_sizes[3] != RR * SKV || in_sizes[4] != RR * SKV || out_size != BH * DH || ws_size < WS_END)
    return;
  const uint16_t* q = (const uint16_t*)d_in[0];
  const uint16_t* k = (const uint16_t*)d_in[1];
  const uint16_t* v = (const uint16_t*)d_in[2];
  const uint16_t* e = (const uint16_t*)d_in[3];
  const uint16_t* f = (const uint16_t*)d_in[4];
  float* ps = (float*)((char*)d_ws + WS_PS);
  float* po = (float*)((char*)d_ws + WS_PO);
  kphase_kernel<<<BH * S1, 64 * NW1, 0, stream>>>(q, k, e, ps);
  vphase_kernel<<<BH * S2, 64 * NW2, 0, stream>>>(v, f, ps, po);
  combine_kernel<<<BH, DH, 0, stream>>>(po, (__hip_bfloat16*)d_out);
}
```
